```python
import torch

def _softmax(x, axis=-1):
    return torch.softmax(x, dim=axis)
def _gelu(x):
    return torch.nn.functional.gelu(x, approximate="tanh")
def _silu(x):
    return torch.nn.functional.silu(x)
def _sigmoid(x):
    return torch.sigmoid(x)
def _layernorm(x, axis=-1, eps=1e-5):
    m = x.mean(dim=axis, keepdim=True)
    v = x.var(dim=axis, keepdim=True, unbiased=False)
    return (x - m) / torch.sqrt(v + eps)
def _scatter_add(tgt, idx, src, axis=0):
    return tgt.clone().index_add_(axis, idx, src)
def _segment_sum(src, seg, n):
    out = torch.zeros((n,) + src.shape[1:], dtype=src.dtype)
    return out.index_add_(0, seg, src)
def _pad(x, pw):
    flat = tuple(v for pair in reversed(pw) for v in pair)
    return torch.nn.functional.pad(x, flat)

kernel_name = 'gated_mix__linear_kernel_hedgehog__swiglu_ffn__prompt'

def setup_inputs():

    _w = (lambda a: (lambda t: t.to(torch.bfloat16) if t.is_floating_point() else t)(torch.as_tensor(a)))
    def _A_setup():
        B, S, D, H = 1, 2048, 640, 32
        Dh = D // H
        return {
            "x": _w((lambda *s: torch.randn(*s, dtype=torch.bfloat16) * 0.05)(B, S, D)),
            "wqkv": _w((lambda *s: torch.randn(*s, dtype=torch.bfloat16) * 0.8)(D, 3 * D)),
            "w_phi": _w((lambda *s: torch.randn(*s, dtype=torch.bfloat16) * 0.8)(H, Dh, Dh) * (Dh ** -0.5)),
            "wo": _w((lambda *s: torch.randn(*s, dtype=torch.bfloat16) * 0.05)(D, D)),
        }
    _A_in = _A_setup()

    def _B_setup():
        B, S, D, F = 1, 2048, 640, 1728
        return {
            "x": _w((lambda *s: torch.randn(*s, dtype=torch.bfloat16) * 0.05)(B, S, D)),
            "w_gate": _w((lambda *s: torch.randn(*s, dtype=torch.bfloat16) * 0.05)(D, F)),
            "w_up": _w((lambda *s: torch.randn(*s, dtype=torch.bfloat16) * 0.05)(D, F)),
            "w_down": _w((lambda *s: torch.randn(*s, dtype=torch.bfloat16) * 0.05)(F, D)),
        }
    _B_in = _B_setup()

    return {
        "A_x": _A_in["x"],
        "A_wqkv": _A_in["wqkv"],
        "A_w_phi": _A_in["w_phi"],
        "A_wo": _A_in["wo"],
        "B_x": _B_in["x"],
        "B_w_gate": _B_in["w_gate"],
        "B_w_up": _B_in["w_up"],
        "B_w_down": _B_in["w_down"],
        "gate_w": _w((lambda *s: torch.randn(*s, dtype=torch.bfloat16) * 0.05)(640) * 16.0),
    }

def reference(A_x, A_wqkv, A_w_phi, A_wo, B_x, B_w_gate, B_w_up, B_w_down, gate_w):
    def _A_ref(x, wqkv, w_phi, wo):
        B, S, D = x.shape
        H = 32; Dh = D // H
        qkv = (lambda x, *d: x.permute(*d))((x @ wqkv).reshape(B, S, 3, H, Dh), 2, 0, 3, 1, 4)
        q, k, v = qkv[0], qkv[1], qkv[2]
        pq = torch.einsum("bhsd,hdf->bhsf", q, w_phi)
        pk = torch.einsum("bhsd,hdf->bhsf", k, w_phi)
        phi_q = torch.concatenate([_softmax(pq, axis=-1), _softmax(-pq, axis=-1)], axis=-1)
        phi_k = torch.concatenate([_softmax(pk, axis=-1), _softmax(-pk, axis=-1)], axis=-1)
        kv = torch.einsum("bhsf,bhsd->bhfd", phi_k, v)
        z  = torch.einsum("bhsf->bhf", phi_k)
        num = torch.einsum("bhqf,bhfd->bhqd", phi_q, kv)
        den = torch.einsum("bhqf,bhf->bhq", phi_q, z)[..., None] + 1e-6
        o = num / den
        return (lambda x, *d: x.permute(*d))(o, 0, 2, 1, 3).reshape(B, S, D) @ wo

    def _B_ref(x, w_gate, w_up, w_down):
        g = _silu(x @ w_gate)
        u = x @ w_up
        return (g * u) @ w_down

    x = A_x
    g = _sigmoid((x * gate_w).sum(-1, keepdims=True))
    a = _A_ref(x, A_wqkv, A_w_phi, A_wo)
    b = _B_ref(x, B_w_gate, B_w_up, B_w_down)
    return g * a + (1 - g) * b
```

```cpp
#include <hip/hip_runtime.h>
#include <hip/hip_bf16.h>
typedef unsigned short u16;
__device__ __forceinline__ float b2f(u16 v){ return __uint_as_float(((unsigned)v)<<16); }
__device__ __forceinline__ u16 f2b(float f){ unsigned u=__float_as_uint(f); u += 0x7fffu + ((u>>16)&1u); return (u16)(u>>16); }
constexpr int S=2048,D=640,H=32,DH=20,F=1728;
__global__ void gemm_naive(const u16* A,const u16* B,float* C,int M,int N,int K,int ldb){
  int n=blockIdx.x*blockDim.x+threadIdx.x, m=blockIdx.y; if(n>=N) return;
  float acc=0; for(int k=0;k<K;++k) acc+=b2f(A[(long)m*K+k])*b2f(B[(long)k*ldb+n]);
  C[(long)m*N+n]=acc;
}
__global__ __launch_bounds__(128) void phi_k(const float* qkv,const u16* wphi,float* phiq,float* phik){
  int idx=blockIdx.x*blockDim.x+threadIdx.x;
  int which=idx&1; int h=(idx>>1)%H; int s=(idx>>1)/H; if(s>=S) return;
  const float* q=qkv+(long)s*1920+which*640+h*DH;
  float p[DH]; float mx=-1e30f,mn=1e30f;
  #pragma unroll
  for(int f=0;f<DH;++f){ float a=0;
    #pragma unroll
    for(int d=0;d<DH;++d) a+=b2f(f2b(q[d]))*b2f(wphi[(h*DH+d)*DH+f]); p[f]=a; mx=fmaxf(mx,a); mn=fminf(mn,a);}
  float s1=0,s2=0; float e1[DH],e2[DH];
  #pragma unroll
  for(int f=0;f<DH;++f){ e1[f]=__expf(p[f]-mx); e2[f]=__expf(mn-p[f]); s1+=e1[f]; s2+=e2[f];}
  float* o=(which?phik:phiq)+((long)s*H+h)*40;
  #pragma unroll
  for(int f=0;f<DH;++f){ o[f]=e1[f]/s1; o[20+f]=e2[f]/s2;}
}
__global__ void kv_k(const float* qkv,const float* phik,float* kv){
  int h=blockIdx.x; int t=threadIdx.x; if(t>=840) return; int f=t/21,d=t%21;
  float a=0; for(int s=0;s<S;++s){ float v= d<20? qkv[(long)s*1920+1280+h*DH+d]:1.f; a+=phik[((long)s*H+h)*40+f]*v;}
  kv[h*840+t]=a;
}
__global__ void o_k(const float* phiq,const float* kv,u16* o){
  int idx=blockIdx.x*blockDim.x+threadIdx.x; int c=idx%640,s=idx/640; if(s>=S) return; int h=c/20,d=c%20;
  float num=0,den=0; for(int f=0;f<40;++f){ float p=phiq[((long)s*H+h)*40+f]; num+=p*kv[h*840+f*21+d]; den+=p*kv[h*840+f*21+20];}
  o[idx]=f2b(num/(den+1e-6f));
}
__global__ void h_k(const float* g,const float* u,u16* h){
  long idx=(long)blockIdx.x*blockDim.x+threadIdx.x; if(idx>=(long)S*F) return; float a=g[idx]; h[idx]=f2b(a/(1.f+__expf(-a))*u[idx]);
}
__global__ void fin_k(const u16* x,const u16* gw,const float* a,const float* b,u16* out){
  int s=blockIdx.x; __shared__ float red[64]; int t=threadIdx.x; float acc=0;
  for(int k=t;k<640;k+=64) acc+=b2f(x[s*640+k])*b2f(gw[k]);
  red[t]=acc; __syncthreads(); float tot=0; for(int i=0;i<64;++i) tot+=red[i];
  float g=1.f/(1.f+__expf(-tot));
  for(int c=t;c<640;c+=64) out[s*640+c]=f2b(g*a[s*640+c]+(1.f-g)*b[s*640+c]);
}
extern "C" void kernel_launch(void* const* d_in, const int* in_sizes, int n_in, void* d_out, int out_size, void* d_ws, size_t ws_size, hipStream_t stream){
  const u16* x=(const u16*)d_in[0]; const u16* wqkv=(const u16*)d_in[1]; const u16* wphi=(const u16*)d_in[2]; const u16* wo=(const u16*)d_in[3];
  const u16* wg=(const u16*)d_in[5]; const u16* wu=(const u16*)d_in[6]; const u16* wd=(const u16*)d_in[7]; const u16* gw=(const u16*)d_in[8];
  char* ws=(char*)d_ws; size_t MB=1<<20;
  float* qkv=(float*)(ws); float* phiq=(float*)(ws+16*MB); float* phik=(float*)(ws+32*MB); float* kv=(float*)(ws+48*MB);
  u16* o=(u16*)(ws+50*MB); float* g=(float*)(ws+64*MB); float* u=(float*)(ws+80*MB); u16* h=(u16*)(ws+96*MB); float* a=(float*)(ws+112*MB); float* b=(float*)(ws+128*MB);
  gemm_naive<<<dim3(1920/128,S),128,0,stream>>>(x,wqkv,qkv,S,1920,640,1920);
  phi_k<<<S*H*2/128,128,0,stream>>>(qkv,wphi,phiq,phik);
  kv_k<<<H,896,0,stream>>>(qkv,phik,kv);
  o_k<<<S*640/128,128,0,stream>>>(phiq,kv,o);
  gemm_naive<<<dim3(640/128,S),128,0,stream>>>(o,wo,a,S,640,640,640);
  gemm_naive<<<dim3((F+127)/128,S),128,0,stream>>>(x,wg,g,S,F,640,F);
  gemm_naive<<<dim3((F+127)/128,S),128,0,stream>>>(x,wu,u,S,F,640,F);
  h_k<<<S*F/128,128,0,stream>>>(g,u,h);
  gemm_naive<<<dim3(640/128,S),128,0,stream>>>(h,wd,b,S,640,F,640);
  fin_k<<<S,64,0,stream>>>(x,gw,a,b,(u16*)d_out);
}
```

```cpp
#define ROT 3
#define ROTA 3
#include <hip/hip_runtime.h>
#include <stdint.h>

#define SPLIT 0
#define STAMPS 0

typedef short s16x4 __attribute__((ext_vector_type(4)));
typedef short s16x8 __attribute__((ext_vector_type(8)));
typedef float f32x2 __attribute__((ext_vector_type(2)));
typedef float f32x4 __attribute__((ext_vector_type(4)));
typedef float f32x16 __attribute__((ext_vector_type(16)));
typedef unsigned u32x2 __attribute__((ext_vector_type(2)));
typedef unsigned u32x4 __attribute__((ext_vector_type(4)));
typedef __bf16 bf16x2 __attribute__((ext_vector_type(2)));
typedef unsigned long long u64;
#define LAS __attribute__((address_space(3)))
#define GAS __attribute__((address_space(1)))
#define DI __device__ __forceinline__
typedef __amdgpu_buffer_rsrc_t rsrc_t;

constexpr int S = 2048, D = 640, F = 1728;
#define SPAD 0
constexpr int SP = S + SPAD;
constexpr int OFF_E = 0, OFF_AF = 1024, OFF_MF = 8192, OFF_GF = 10240, OFF_PF = 10368, OFF_HF = 10752;
constexpr int OFF_G = 20480, OFF_KVP = 32768, KVP_STRIDE = 6912;
constexpr int OFF_O = 1 << 20, OFF_H = 4 << 20, OFF_ST = 12 << 20, WS_USED = 13 << 20;
constexpr int LDS_BYTES = 160 * 1024;
constexpr int AUX_SC1 = 16;
#define DBG 0
#define AUXA 16

struct Params {
  const char* x; const char* wqkv; const char* wphi; const char* wo;
  const char* wg; const char* wu; const char* wd; const char* gw;
  char* out; char* ws;
};

DI rsrc_t mk_rsrc(const void* p, unsigned bytes) { return __builtin_amdgcn_make_buffer_rsrc((void*)p, 0, bytes, 0x00020000); }
typedef int srd_t __attribute__((ext_vector_type(4)));
DI srd_t mk_srd(const void* p, unsigned bytes) {
  const u64 a = (u64)p;
  return (srd_t){__builtin_amdgcn_readfirstlane((int)(unsigned)a), __builtin_amdgcn_readfirstlane((int)((unsigned)(a >> 32) & 0xffffu)), (int)bytes, 0x00020000};
}
DI void glds16(srd_t r, LAS char* dst, int voff, int soff) {
  asm volatile("s_mov_b32 m0, %0\n\ts_nop 0\n\tbuffer_load_dwordx4 %1, %2, %3 offen lds" ::"s"((unsigned)(uintptr_t)dst), "v"(voff), "s"(r), "s"(soff) : "memory");
}
DI void glds4(srd_t r, LAS char* dst, int voff, int soff) {
  asm volatile("s_mov_b32 m0, %0\n\ts_nop 0\n\tbuffer_load_dword %1, %2, %3 offen lds" ::"s"((unsigned)(uintptr_t)dst), "v"(voff), "s"(r), "s"(soff) : "memory");
}
DI s16x8 lds_r128(LAS char* p) { return *(LAS s16x8*)p; }
DI s16x4 lds_tr(LAS char* p) { return __builtin_amdgcn_ds_read_tr16_b64_v4i16((LAS s16x4*)p); }
DI s16x8 cat(s16x4 a, s16x4 b) { return __builtin_shufflevector(a, b, 0, 1, 2, 3, 4, 5, 6, 7); }
DI f32x4 mfma32(s16x8 a, s16x8 b, f32x4 c) { return __builtin_amdgcn_mfma_f32_16x16x32_bf16(a, b, c, 0, 0, 0); }
DI f32x16 mfmaw(s16x8 a, s16x8 b, f32x16 c) { return __builtin_amdgcn_mfma_f32_32x32x16_bf16(a, b, c, 0, 0, 0); }
DI f32x4 mfma16(s16x4 a, s16x4 b, f32x4 c) { return __builtin_amdgcn_mfma_f32_16x16x16bf16_1k(a, b, c, 0, 0, 0); }
DI unsigned pk2(float a, float b) { f32x2 v = {a, b}; return __builtin_bit_cast(unsigned, __builtin_convertvector(v, bf16x2)); }
DI float blo(unsigned u) { return __uint_as_float(u << 16); }
DI float bhi(unsigned u) { return __uint_as_float(u & 0xffff0000u); }
template <int V> struct IC { static constexpr int value = V; };
template <int B, int E, class Fn> DI void static_for(Fn&& f) { if constexpr (B < E) { f(IC<B>{}); static_for<B + 1, E>(f); } }
#define SBAR() __builtin_amdgcn_sched_barrier(0)
#define CBAR() asm volatile("" ::: "memory")
#define WAIT_VM(n) asm volatile("s_waitcnt vmcnt(" #n ")" ::: "memory")
#define WAIT_LGKM0() asm volatile("s_waitcnt lgkmcnt(0)" ::: "memory")
DI int lane_id() { int l; asm volatile("v_mbcnt_lo_u32_b32 %0, -1, 0\n\tv_mbcnt_hi_u32_b32 %0, -1, %0" : "=v"(l)); return l; }
DI f32x4 sel4(bool c, f32x4 a, f32x4 b) { return c ? a : b; }
DI void bar() { CBAR(); __builtin_amdgcn_s_barrier(); CBAR(); }
template <int N> DI void wait_vm() { asm volatile("s_waitcnt vmcnt(%0)" ::"n"(N) : "memory"); }
DI void wait_vmc(int n) { asm volatile("s_waitcnt vmcnt(%0)" ::"i"(n) : "memory"); }
constexpr int L_EPOCH = 163584, L_DBG = 158720, L_DUMP = 162816, L_FLG = 161792, L_FOFF = 161280;
DI void fetch_epoch(const char* ws, LAS char* L, int wid, int lane) {
  if (wid == 0)
    asm volatile("s_mov_b32 m0, %0\n\ts_nop 0\n\tbuffer_load_dword %1, %2, 0 offen sc1 lds" ::"s"((unsigned)(uintptr_t)(L + L_EPOCH)), "v"(OFF_E + lane * 4), "s"(mk_srd(ws, 4096)) : "memory");
}
DI void flag_fetch(const char* ws, LAS char* dst, int off) {
  const srd_t r = mk_srd(ws, 32768);
  asm volatile("s_mov_b32 m0, %0\n\ts_nop 0\n\tbuffer_load_dword %1, %2, 0 offen sc1 lds" ::"s"((unsigned)(uintptr_t)dst), "v"(off), "s"(r) : "memory");
  asm volatile("s_mov_b32 m0, %0\n\ts_nop 0\n\tbuffer_load_dword %1, %2, 0 offen sc1 lds" ::"s"((unsigned)(uintptr_t)(dst + 256)), "v"(off + 4), "s"(r) : "memory");
}
DI bool flag_seen(LAS char* src, int lane, u64 tag) {
  const unsigned lo = *(LAS unsigned*)(src + lane * 4), hi = *(LAS unsigned*)(src + 256 + lane * 4);
  return __all(lo == (unsigned)tag && hi == (unsigned)(tag >> 32));
}
DI u64 make_tag(LAS char* L) {
  const u64 e0 = *(LAS u64*)(L + L_EPOCH);
  const unsigned lo = __builtin_amdgcn_readfirstlane((unsigned)e0), hi = __builtin_amdgcn_readfirstlane((unsigned)(e0 >> 32));
  return (((u64)hi << 32) | lo) + 0x9E3779B97F4A7C15ull;
}
DI void drain_vm() { asm volatile("s_waitcnt vmcnt(0)" ::: "memory"); }
DI u64 ld_flag(const char* p) { return __hip_atomic_load((const GAS u64*)p, __ATOMIC_RELAXED, __HIP_MEMORY_SCOPE_AGENT); }
DI void st_flag(char* p, u64 v) { __hip_atomic_store((GAS u64*)p, v, __ATOMIC_RELAXED, __HIP_MEMORY_SCOPE_AGENT); }
DI void poll(const char* ws, int off, u64 tag) {
#pragma nounroll
  for (unsigned it = 0; it < (1u << 17); ++it) {
    const u64 v = ld_flag(ws + off);
    if (__all(v == tag)) break;
    __builtin_amdgcn_s_sleep(2);
  }
  CBAR();
}
#define STAMP2(n)
#define DUMP2()
#define STAMP(k)

constexpr int W2_SLOTS = 18, W2_STAGES = 37, W2_PRE = 16;
constexpr int AS = 8;
DI constexpr int w2_slot(int n) { return ((n + 10) % W2_SLOTS) * 8192; }
struct P2 {
  srd_t wd, wo;
  int row0, nm, c, ra, ro;
  bool on;
  int ta[9], tw[9];
};
DI int p2_stage(const P2& p, int n) {
  if (n < 27) { int a = n % 9 + p.ra; a = a >= 9 ? a - 9 : a; return 3 * a + n / 9; }
  int o = n - 27 + p.ro; o = o >= 10 ? o - 10 : o; return o;
}
DI int w2_voff(const P2& p, int wid, int lane) {
  const int u = lane & 7;
  return (16 * wid + (lane >> 3)) * 1280 + ((((u >> 1) ^ ((lane >> 4) & 1)) << 1) | (u & 1)) * 16 + (p.on ? p.c * 128 : 0x40000000);
}
DI void w2_piece(const P2& p, LAS char* L, int n, int wid, int e, int v0, int soff) {
  glds16(n < 27 ? p.wd : p.wo, L + w2_slot(n) + (2 * wid + e) * 1024, e ? (v0 ^ 64) + 8 * 1280 : v0, soff);
}
struct AFrag { u32x4 v[AS + 1][3]; };
DI int p2_tok(const P2& p, int wid) { return p.row0 + 16 * p.nm * (wid >> 1); }
DI int a_voff(const P2& p, int wid, int lane) {
  return (((wid & 1) * 4 + (lane >> 4)) * SP + (lane & 15)) * 16 + ((p.on && wid < 4 && p2_tok(p, wid) < S) ? p2_tok(p, wid) * 16 : 0x40000000);
}
DI int p2_flag(const P2& p, int lane, int unit, int hg = -1) {
  const int rl0 = p.row0 + 32 * p.nm - 1, rl = rl0 < S ? rl0 : S - 1;
  if (hg >= 0 && (lane >= 50 || !p.on)) return OFF_MF + (lane % 13) * 128 + hg * 8;
  if (lane >= 32) return OFF_GF + ((lane & 1) ? p.row0 >> 8 : rl >> 8) * 8;
  const int j = lane & 15;
  return OFF_HF + ((lane & 16) ? rl >> 7 : p.row0 >> 7) * 384 + unit * 128 + (j < 9 ? j : 0) * 8;
}
DI unsigned umax3(unsigned a, unsigned b, unsigned c) { return __builtin_elementwise_max(__builtin_elementwise_max(a, b), c); }
template <int N> DI void p2_table(const P2& p, int* t, int n0, int mul) {
#pragma unroll
  for (int j = 0; j < N; ++j) { t[j] = p2_stage(p, n0 + j) * mul; asm volatile("" : "+s"(t[j])); }
}

constexpr int F_W = 0, F_X = 81920;
DI constexpr int ffn_ops(int n) {
  return (n + 4 >= 0 && n + 4 < 30 ? 2 : 0) + (n + 3 >= 0 && n + 3 < 10 ? 2 : 0) + (n == 10 ? 4 : n == 20 ? 2 : 0) + (n > 10 && n <= 18 ? 1 : 0) + (n >= 22 ? 3 : 0);
}
DI constexpr int ffn_wait(int n) { return n == 0 ? 4 : n == 1 ? 6 : n < 4 ? 10 : ffn_ops(n - 1) + ffn_ops(n - 2) + (n < 10 ? 0 : ffn_ops(n - 3)); }

DI void silu_store(const f32x4 (&acc)[4][2], rsrc_t rh, int vh, int soff) {
#pragma unroll
  for (int n = 0; n < 2; ++n) {
    unsigned w[2][2];
#pragma unroll
    for (int m = 0; m < 2; ++m) {
#pragma unroll
      for (int r = 0; r < 4; r += 2) {
        const f32x2 gt = {acc[2 * n][m][r], acc[2 * n][m][r + 1]}, up = {acc[2 * n + 1][m][r], acc[2 * n + 1][m][r + 1]};
        const f32x2 z = gt * -1.44269504089f;
        const f32x2 d = (f32x2){__builtin_amdgcn_exp2f(z[0]), __builtin_amdgcn_exp2f(z[1])} + 1.f;
        const f32x2 h = gt * (f32x2){__builtin_amdgcn_rcpf(d[0]), __builtin_amdgcn_rcpf(d[1])} * up;
        w[m][r >> 1] = pk2(h[0], h[1]);
      }
    }
    const auto r0 = __builtin_amdgcn_permlane16_swap(w[0][0], w[1][0], false, false);
    const auto r1 = __builtin_amdgcn_permlane16_swap(w[0][1], w[1][1], false, false);
    __builtin_amdgcn_raw_buffer_store_b128((u32x4){r0[0], r1[0], r0[1], r1[1]}, rh, vh + soff + n * 2 * SP * 16, 0, AUX_SC1);
  }
}

DI void ffn_block(const Params& P, LAS char* L, int bi, int bj, int wid, int lane, const P2& p2, u64& tag, AFrag& A) {
  const srd_t rx = mk_srd(P.x, S * D * 2), rg = mk_srd(P.wg, D * F * 2), ru = mk_srd(P.wu, D * F * 2);
  const int rot = ROT == 0 ? 0 : ROT == 1 ? ((bi & 7) + 3 * (bj % 3)) % 10 : ROT == 2 ? 5 * ((bi + bj) & 1) : (3 * ((bi + bj) & 3)) % 10;
  const int vx = (bi * 128 + 8 * wid + (lane >> 3)) * 1280 + (((lane & 7) ^ ((lane >> 4) | ((wid & 1) << 2))) << 4);
  const int vw = (8 * wid + (lane >> 3)) * 3456 + bj * 384 + (((((lane & 7) >> 1) ^ (((lane >> 4) & 1) | ((wid & 1) << 1))) << 1) | (lane & 1)) * 16;
  int sx = rot * 128, sw1 = rot * 64 * 3456;
  const int vx1 = vx + 64 * 1280;
  auto ix = [&](int n, int e) {
    if (n < 10) {
      glds16(rx, L + F_X + (n & 3) * 16384 + (e * 8 + wid) * 1024, e ? vx1 : vx, sx);
      if (e) { sx = sx == 9 * 128 ? 0 : sx + 128; asm volatile("" : "+s"(sx)); }
    }
  };
  auto iw = [&](int n, int e) {
    if (n < 30) {
      glds16(e ? ru : rg, L + F_W + (n % 5) * 16384 + (e * 8 + wid) * 1024, vw, sw1);
      if (e) { sw1 = (sw1 >= 9 * 64 * 3456 ? sw1 - 9 * 64 * 3456 : sw1 + 64 * 3456) + (n % 10 == 9 ? 128 : 0); asm volatile("" : "+s"(sw1)); }
    }
  };
  iw(0, 0); ix(0, 0); iw(0, 1); ix(0, 1);
  iw(1, 0); iw(1, 1); ix(1, 0); ix(1, 1);
  if (wid == 7) { *(LAS int*)(L + L_FOFF + lane * 4) = p2_flag(p2, lane, 0); *(LAS int*)(L + L_FOFF + 256 + lane * 4) = p2_flag(p2, lane, 1); }

  const int wm = wid & 3, wn = wid >> 2, t = lane & 15, g = lane >> 4, q = (lane >> 2) & 3, p = lane & 3;
  LAS char* xa = L + F_X + (32 * wm + t) * 128;
  const int xs0 = (g ^ (t >> 1)) << 4;
  LAS char* wa[2];
  {
    const int sw = ((q >> 1) & 1) | ((g & 1) << 1);
#pragma unroll
    for (int n = 0; n < 2; ++n) wa[n] = L + F_W + (8 * g + q) * 128 + (((2 * wn + n) ^ sw) << 5) + 8 * p;
  }
  const rsrc_t rh = mk_rsrc(P.ws + OFF_H, F * SP * 2);
  auto vh = [&]() { const int l = lane_id(); return ((bj * 24 + wn * 4 + (l >> 5)) * SP + bi * 128 + 32 * wm + (l & 31)) * 16; };
  int v2 = 0, va = 0;
  const bool ldw = wid >= 4;
  auto ring = [&](int pos, int e) { w2_piece(p2, L, pos, wid & 3, e, pos < 9 ? v2 : v2 + 64 * 1280, p2.tw[pos % 9]); };
  auto ring1 = [&](int pos) { glds16(p2.wd, L + w2_slot(pos) + (2 * (wid & 3) + (wid >> 2)) * 1024, va, p2.tw[pos]); };
  auto lda = [&](int pos, int m) { A.v[pos][m] = __builtin_amdgcn_raw_buffer_load_b128(rh, va + m * 256, p2.ta[pos], AUX_SC1); };
  const f32x4 zero = {0.f, 0.f, 0.f, 0.f};
  f32x4 acc[4][2];
  s16x8 xr[20][2];
  s16x4 wl[5], wh[5];
  auto rd = [&](int n, int f, int h) {
    LAS char* b = wa[(f >> 1) & 1] + (n % 5) * 16384 + (f & 1) * 8192 + (f >> 2) * 4096;
    if (h) wh[(8 * n + f) % 5] = lds_tr(b + 512); else wl[(8 * n + f) % 5] = lds_tr(b);
    SBAR();
  };
  auto mm = [&](int n, int f, int m) {
    if (n >= 0) acc[f & 3][m] = mfma32(cat(wl[(8 * n + f) % 5], wh[(8 * n + f) % 5]), xr[2 * (n % 10) + (f >> 2)][m], (n % 10 == 0 && f < 4) ? zero : acc[f & 3][m]);
    SBAR();
  };
  auto rx2 = [&](int n, int kk, int m) { if (n < 10) { xr[2 * n + kk][m] = lds_r128(xa + (n & 3) * 16384 + m * 2048 + (xs0 ^ (64 * kk))); SBAR(); } };
  if (wid >= 4) __builtin_amdgcn_s_setprio(1);
  STAMP(1);
  static_for<0, 30>([&](auto NN) __attribute__((always_inline)) {
    constexpr int n = decltype(NN)::value;
    wait_vmc(ffn_wait(n));
    if (n == 22 && wid == 7) {
      if (!flag_seen(L + L_FLG, lane_id(), tag)) poll(P.ws, p2_flag(p2, lane_id(), 0), tag);
    }
    WAIT_LGKM0();
    bar();
    STAMP2(n);
    if (n == 0) STAMP(13);
    if (n == 10) STAMP(14);
    if (n == 20) STAMP(15);
    if ((n == 14 || n == 24) && wid == 0 && lane_id() == 0) st_flag(P.ws + OFF_HF + bi * 384 + (n / 10 - 1) * 128 + bj * 8, tag);
    if ((n == 18 || n == 27) && wid == 7) flag_fetch(P.ws, L + L_FLG + (n == 27 ? 512 : 0), *(LAS int*)(L + L_FOFF + (n == 27 ? 256 : 0) + lane_id() * 4));
    SBAR();
    rd(n, 0, 0); rd(n, 0, 1);
    mm(n - 1, 5, 0); rx2(n, 0, 0); rd(n, 1, 0); mm(n - 1, 5, 1); rx2(n, 0, 1); rd(n, 1, 1);
    mm(n - 1, 6, 0); rd(n, 2, 0); mm(n - 1, 6, 1); rd(n, 2, 1);
    if (n == 0) { iw(2, 0); iw(2, 1); ix(2, 0); ix(2, 1); } else if (n == 1) { ix(3, 0); ix(3, 1); iw(4, 0); iw(4, 1); }
    iw(n == 0 ? 3 : n + 4, 0); SBAR();
    mm(n - 1, 7, 0); rd(n, 3, 0); rx2(n, 1, 0); mm(n - 1, 7, 1); rd(n, 3, 1); rx2(n, 1, 1);
    if (n == 0) {
      tag = make_tag(L);
      if (wid == 0 && lane_id() == 0) st_flag(P.ws + OFF_AF + blockIdx.x * 8, tag);
      SBAR();
    }
    if (n == 10 || n == 20) { rd(n, 4, 0); rd(n, 4, 1); iw(n + 4, 1); SBAR(); silu_store(acc, rh, vh(), (n / 10 - 1) * 8 * SP * 16); SBAR(); }
    if (n == 10) {
      const int v = vh() + 2 * 8 * SP * 16;
      __builtin_amdgcn_raw_buffer_store_b128((u32x4){~0u, ~0u, ~0u, ~0u}, rh, v, 0, AUX_SC1);
      __builtin_amdgcn_raw_buffer_store_b128((u32x4){~0u, ~0u, ~0u, ~0u}, rh, v + 2 * SP * 16, 0, AUX_SC1);
    }
    if (n == 10) { va = w2_voff(p2, wid & 3, lane_id()); va = ldw ? (va ^ 64) + 8 * 1280 : va; }
    if (n == 20) { va = a_voff(p2, wid, lane_id()); v2 = w2_voff(p2, wid & 3, lane_id()); }
    if (n == 10 || n == 20) { mm(n, 0, 0); mm(n, 0, 1); }
    else { mm(n, 0, 0); rd(n, 4, 0); mm(n, 0, 1); rd(n, 4, 1); iw(n == 0 ? 3 : n + 4, 1); SBAR(); }
    mm(n, 1, 0); rd(n, 5, 0); mm(n, 1, 1); rd(n, 5, 1);
    if (n > 0) ix(n + 3, 0);
    if (n > 10 && n <= 18) ring1(n - 11);
    if (n >= 22) lda(n - 22, 0);
    if (n >= 26 && ldw) { ring(2 * n - 44, 0); ring(2 * n - 44, 1); }
    SBAR();
    mm(n, 2, 0); rd(n, 6, 0); mm(n, 2, 1); rd(n, 6, 1);
    if (n >= 22) { lda(n - 22, 1); SBAR(); }
    mm(n, 3, 0); rd(n, 7, 0); mm(n, 3, 1); rd(n, 7, 1);
    if (n > 0) ix(n + 3, 1);
    if (n >= 22) lda(n - 22, 2);
    SBAR();
    mm(n, 4, 0);
    if (n >= 26 && ldw) { ring(2 * n - 43, 0); ring(2 * n - 43, 1); SBAR(); }
    mm(n, 4, 1);
  });
  mm(29, 5, 0); mm(29, 5, 1); mm(29, 6, 0); mm(29, 6, 1); mm(29, 7, 0); mm(29, 7, 1);
  __builtin_amdgcn_s_setprio(0);
  STAMP(2);
  silu_store(acc, rh, vh(), 2 * 8 * SP * 16);
  STAMP(3);
}

DI float max3(float a, float b, float c) { return __builtin_fmaxf(__builtin_fmaxf(a, b), c); }
DI float maxr(unsigned a, unsigned b) { float r; asm("v_max_f32 %0, %1, %2" : "=v"(r) : "v"(a), "v"(b)); return r; }
template <int NM, class Hook>
DI void dual_softmax(const f32x16* u, const float* valid, unsigned (*o)[10], Hook&& hook) {
  constexpr float LG = 1.44269504089f;
  unsigned x[NM], y[NM];
  f32x2 c[NM], e[NM][10];
#pragma unroll
  for (int m = 0; m < NM; ++m) {
    x[m] = __float_as_uint(max3(max3(max3(max3(u[m][0], u[m][1], u[m][2]), u[m][3], u[m][4]), u[m][5], u[m][6]), max3(u[m][7], u[m][8], u[m][9]), u[m][0]));
    y[m] = __float_as_uint(max3(max3(max3(max3(-u[m][0], -u[m][1], -u[m][2]), -u[m][3], -u[m][4]), -u[m][5], -u[m][6]), max3(-u[m][7], -u[m][8], -u[m][9]), -u[m][0]));
  }
  SBAR();
#pragma unroll
  for (int m = 0; m < NM; ++m) { const auto r = __builtin_amdgcn_permlane32_swap(x[m], y[m], false, false); x[m] = __float_as_uint(maxr(r[0], r[1]) * -LG); }
  SBAR();
#pragma unroll
  for (int m = 0; m < NM; ++m) { const auto r = __builtin_amdgcn_permlane32_swap(x[m], x[m], false, false); c[m] = (f32x2){__uint_as_float(r[0]), __uint_as_float(r[1])}; }
  static_for<0, NM>([&](auto MM) __attribute__((always_inline)) {
    constexpr int m = decltype(MM)::value;
    static_for<0, 5>([&](auto JJ) __attribute__((always_inline)) {
      constexpr int j = decltype(JJ)::value;
      SBAR(); hook(IC<5 * m + j>{}); SBAR();
#pragma unroll
      for (int i = 2 * j; i < 2 * j + 2; ++i) {
        const f32x2 z = __builtin_elementwise_fma((f32x2){u[m][i], u[m][i]}, (f32x2){LG, -LG}, c[m]);
        e[m][i] = (f32x2){__builtin_amdgcn_exp2f(z[0]), __builtin_amdgcn_exp2f(z[1])};
      }
    });
    c[m] = (((e[m][0] + e[m][1]) + (e[m][2] + e[m][3])) + ((e[m][4] + e[m][5]) + (e[m][6] + e[m][7]))) + (e[m][8] + e[m][9]);
  });
  SBAR();
#pragma unroll
  for (int m = 0; m < NM; ++m) {
    const auto r = __builtin_amdgcn_permlane32_swap(__float_as_uint(c[m][0]), __float_as_uint(c[m][1]), false, false);
    x[m] = __float_as_uint(__builtin_amdgcn_rcpf(__uint_as_float(r[0]) + __uint_as_float(r[1])) * (valid ? valid[m] : 1.f));
  }
  SBAR();
  static_for<0, NM>([&](auto MM) __attribute__((always_inline)) {
    constexpr int m = decltype(MM)::value;
    const auto r = __builtin_amdgcn_permlane32_swap(x[m], x[m], false, false);
    const f32x2 rr = {__uint_as_float(r[0]), __uint_as_float(r[1])};
    static_for<0, 2>([&](auto JJ) __attribute__((always_inline)) {
      constexpr int j = decltype(JJ)::value;
#pragma unroll
      for (int i = 5 * j; i < 5 * j + 5; ++i) { const f32x2 n = e[m][i] * rr; o[m][i] = pk2(n[0], n[1]); }
      SBAR(); hook(IC<5 * NM + 2 * m + j>{}); SBAR();
    });
  });
}

DI void att_fetch(const Params& P, int hg, int tid, u32x4 (&v)[13]) {
  const rsrc_t rk = mk_rsrc(P.ws + OFF_KVP, 8 * 13 * KVP_STRIDE);
  const int off = tid < 420 ? hg * 13 * KVP_STRIDE + tid * 16 : 0x40000000;
#pragma unroll
  for (int i = 0; i < 13; ++i) v[i] = __builtin_amdgcn_raw_buffer_load_b128(rk, off, i * KVP_STRIDE, AUX_SC1);
}
template <int NW>
DI void att_finish(const Params& P, LAS char* L, int bi, int hg, int wid, int lane, u32x4 (&v)[13], const unsigned (&pq)[NW][10]) {
  constexpr int KVR = 151552;
  {
    auto marked = [&]() {
      unsigned mx = 0u;
#pragma unroll
      for (int i = 0; i < 13; ++i) mx = umax3(mx, v[i][0], v[i][2]);
      return __any(mx == 0xffffffffu);
    };
    if (marked()) {
      const srd_t rk = mk_srd(P.ws + OFF_KVP, 8 * 13 * KVP_STRIDE);
      const int tid = wid * 64 + lane, off = tid < 420 ? hg * 13 * KVP_STRIDE + tid * 16 : 0x40000000;
      unsigned it = 0;
      do {
        int so = 0;
        asm volatile("" : "+s"(so));
        asm volatile("buffer_load_dwordx4 %0, %14, %15, %13 offen sc1\n\ts_add_u32 %13, %13, 6912\n\t"
               "buffer_load_dwordx4 %1, %14, %15, %13 offen sc1\n\ts_add_u32 %13, %13, 6912\n\t"
               "buffer_load_dwordx4 %2, %14, %15, %13 offen sc1\n\ts_add_u32 %13, %13, 6912\n\t"
               "buffer_load_dwordx4 %3, %14, %15, %13 offen sc1\n\ts_add_u32 %13, %13, 6912\n\t"
               "buffer_load_dwordx4 %4, %14, %15, %13 offen sc1\n\ts_add_u32 %13, %13, 6912\n\t"
               "buffer_load_dwordx4 %5, %14, %15, %13 offen sc1\n\ts_add_u32 %13, %13, 6912\n\t"
               "buffer_load_dwordx4 %6, %14, %15, %13 offen sc1\n\ts_add_u32 %13, %13, 6912\n\t"
               "buffer_load_dwordx4 %7, %14, %15, %13 offen sc1\n\ts_add_u32 %13, %13, 6912\n\t"
               "buffer_load_dwordx4 %8, %14, %15, %13 offen sc1\n\ts_add_u32 %13, %13, 6912\n\t"
               "buffer_load_dwordx4 %9, %14, %15, %13 offen sc1\n\ts_add_u32 %13, %13, 6912\n\t"
               "buffer_load_dwordx4 %10, %14, %15, %13 offen sc1\n\ts_add_u32 %13, %13, 6912\n\t"
               "buffer_load_dwordx4 %11, %14, %15, %13 offen sc1\n\ts_add_u32 %13, %13, 6912\n\t"
               "buffer_load_dwordx4 %12, %14, %15, %13 offen sc1\n\ts_add_u32 %13, %13, 6912\n\t"
               "s_waitcnt vmcnt(0)"
               : "+v"(v[0]), "+v"(v[1]), "+v"(v[2]), "+v"(v[3]), "+v"(v[4]), "+v"(v[5]), "+v"(v[6]), "+v"(v[7]), "+v"(v[8]), "+v"(v[9]), "+v"(v[10]), "+v"(v[11]), "+v"(v[12]), "+s"(so)
               : "v"(off), "s"(rk) : "memory", "scc");
      } while (marked() && ++it < (1u << 14));
    }
  }
  STAMP(6);
  {
    f32x2 sum[4];
#pragma unroll
    for (int e = 0; e < 4; ++e) sum[e] = (f32x2){blo(v[0][e]), bhi(v[0][e])};
#pragma unroll
    for (int i = 1; i < 13; ++i)
#pragma unroll
      for (int e = 0; e < 4; ++e) sum[e] += (f32x2){blo(v[i][e]), bhi(v[i][e])};
    const int tid = wid * 64 + lane;
    *(LAS u32x4*)(tid < 420 ? L + KVR + tid * 16 : L + L_DUMP) = (u32x4){pk2(sum[0][0], sum[0][1]), pk2(sum[1][0], sum[1][1]), pk2(sum[2][0], sum[2][1]), pk2(sum[3][0], sum[3][1])};
  }
  WAIT_LGKM0();
  bar();
  STAMP(12);
  {
    const int hh = wid & 3, hf = lane >> 5, odd = hh & 1, row0 = bi * 160 + (NW == 3 ? 0 : 96);
    const int Q = (lane >> 3) & 3, hr = (lane >> 2) & 1;
    const int dr = Q == 3 ? 20 : 4 * (Q == 2 ? (odd ? 0 : 4) : odd + 2 * hr + Q) + (lane & 3);
    LAS char* kr = L + KVR + hh * 1680 + dr * 80;
    const s16x8 a0 = lds_r128(kr + 16 * hf), a1 = lds_r128(kr + 32 + 16 * hf);
    const u32x2 w2 = *(LAS u32x2*)(kr + 64 + 8 * hf);
    const s16x8 a2 = __builtin_bit_cast(s16x8, (u32x4){w2[0], w2[1], 0u, 0u});
    const f32x16 zero = {0.f, 0.f, 0.f, 0.f, 0.f, 0.f, 0.f, 0.f, 0.f, 0.f, 0.f, 0.f, 0.f, 0.f, 0.f, 0.f};
    const rsrc_t ro = mk_rsrc(P.ws + OFF_O, D * SP * 2);
    const int u0 = 5 * (4 * hg + hh);
    const int vo = (((u0 + odd) / 2 + hf) * SP + row0 + (lane & 31)) * 16;
    const int vs = hf == 0 ? (((u0 + (odd ? 0 : 4)) / 2) * SP + row0 + (lane & 31)) * 16 + 8 * odd : 0x40000000;
    f32x16 d[NW];
#pragma unroll
    for (int m = 0; m < NW; ++m) d[m] = mfmaw(a0, __builtin_bit_cast(s16x8, (u32x4){pq[m][0], pq[m][1], pq[m][2], pq[m][3]}), zero);
#pragma unroll
    for (int m = 0; m < NW; ++m) d[m] = mfmaw(a1, __builtin_bit_cast(s16x8, (u32x4){pq[m][4], pq[m][5], pq[m][6], pq[m][7]}), d[m]);
#pragma unroll
    for (int m = 0; m < NW; ++m) d[m] = mfmaw(a2, __builtin_bit_cast(s16x8, (u32x4){pq[m][8], pq[m][9], 0u, 0u}), d[m]);
#pragma unroll
    for (int m = 0; m < NW; ++m) {
      const float rd = __builtin_amdgcn_rcpf(d[m][12] + 1e-6f);
      unsigned w[6];
#pragma unroll
      for (int i = 0; i < 6; ++i) { const f32x2 n = (f32x2){d[m][2 * i], d[m][2 * i + 1]} * rd; w[i] = pk2(n[0], n[1]); }
      const int mo = row0 + 32 * m < S ? m * 512 : 0x20000000;
      __builtin_amdgcn_raw_buffer_store_b128((u32x4){w[0], w[1], w[2], w[3]}, ro, vo + mo, 0, AUX_SC1);
      __builtin_amdgcn_raw_buffer_store_b64((u32x2){w[4], w[5]}, ro, vs + mo, 0, AUX_SC1);
    }
  }
  STAMP(3);
}

template <int NW>
DI void att_block(const Params& P, LAS char* L, int bi, int hg, int wid, int lane, const P2& p2, u64& tag, AFrag& A) {
  constexpr int XB = 20480, W0 = 61440, WB = 16384, WPHI = 147456;
  const srd_t rx = mk_srd(P.x, S * D * 2);
  const srd_t rw = mk_srd(P.wqkv, D * 1920 * 2);
  const int rot = ROTA == 0 ? 0 : ROTA == 1 ? (3 * bi + hg) % 10 : ROTA == 2 ? 5 * ((bi + hg) & 1) : (3 * ((bi + hg) & 3)) % 10;
  const int vx = (bi * 160 + 8 * wid + (lane >> 3)) * 1280 + (((lane & 7) ^ ((lane >> 4) | ((wid & 1) << 2))) << 4);
  int vw[2];
#pragma unroll
  for (int e = 0; e < 2; ++e) {
    const int row = (wid * 2 + e) * 2 + (lane >> 5), pos = lane & 31;
    const int ph = (row & 3) | (((row >> 3) & 1) << 2);
    int su = pos ^ (2 * ph); su = su < 30 ? su : 29;
    const int mat = su / 10;
    vw[e] = row * 3840 + mat * 1280 + hg * 160 + (su - mat * 10) * 16;
  }
  int sx = rot * 128, sw1 = rot * 64 * 3840;
  const int vxh = vx + 80 * 1280;
  auto issue_x = [&](int xh) {
    if (xh > 19) return;
    LAS char* d = L + ((xh >> 1) % 3) * XB + (xh & 1) * 10240;
    glds16(rx, d + wid * 1024, (xh & 1) ? vxh : vx, sx);
    if (wid < 2) glds16(rx, d + (8 + wid) * 1024, (xh & 1) ? vxh : vx, sx + 64 * 1280);
    if (xh & 1) { sx = sx == 9 * 128 ? 0 : sx + 128; asm volatile("" : "+s"(sx)); }
  };
  auto iw = [&](int st, int e) {
    if (st > 19) return;
    glds16(rw, L + W0 + (st & 3) * WB + (wid * 2 + e) * 1024, vw[e], sw1);
    if (e) { sw1 = sw1 == 19 * 32 * 3840 ? 0 : sw1 + 32 * 3840; asm volatile("" : "+s"(sw1)); }
  };
  {
    const srd_t rp = mk_srd(P.wphi, 32 * 400 * 2);
    if (wid < 4) glds16(rp, L + WPHI + wid * 1024, hg * 3200 + wid * 1024 + lane * 16, 0);
  }
  issue_x(0); iw(0, 0); iw(0, 1); issue_x(1); iw(1, 0); iw(1, 1);
  if (wid == 7) { *(LAS int*)(L + L_FOFF + lane * 4) = p2_flag(p2, lane, 0, hg); *(LAS int*)(L + L_FOFF + 256 + lane * 4) = p2_flag(p2, lane, 1, hg); }

  constexpr int M0 = NW == 3 ? 0 : 3;
  const int hh = wid & 3, hf = lane >> 5;
  LAS char* xa = L + (32 * M0 + (lane & 31)) * 128;
  const int xs0 = (hf ^ ((lane >> 1) & 7)) << 4;
  LAS char* wa[2];
#pragma unroll
  for (int T = 0; T < 2; ++T) {
    const int q = (lane >> 2) & 3, pos = 4 * ((lane >> 4) & 1) + (lane & 3);
    const int off = 40 * hh + (pos < 5 ? (T ? 0 : 160) + 8 * pos : 320 + 8 * (T ? (pos == 5 ? 3 : 4) : pos - 5));
    wa[T] = L + W0 + (8 * hf + q) * 512 + (((off >> 4) ^ (2 * (q | (hf << 2)))) << 4) + (off & 8);
  }
  const f32x16 zero = {0.f, 0.f, 0.f, 0.f, 0.f, 0.f, 0.f, 0.f, 0.f, 0.f, 0.f, 0.f, 0.f, 0.f, 0.f, 0.f};
  f32x16 acc[2][NW];
  s16x8 xf[2][NW], ap0, ap1;
  s16x4 wl[2][2], wh[2][2];
  auto rdw = [&](int s, int ks, int T) { LAS char* b = wa[T] + (s & 3) * WB + ks * 8192; wl[ks][T] = lds_tr(b); wh[ks][T] = lds_tr(b + 2048); SBAR(); };
  auto rdx = [&](int s, int ks, int m) { if (m < NW) { xf[ks][m] = lds_r128(xa + ((s >> 1) % 3) * XB + m * 4096 + (xs0 ^ (32 * (2 * (s & 1) + ks)))); SBAR(); } };
  auto mm = [&](int s, int ks, int m, int T) { if (s >= 0 && m < NW) { acc[T][m] = mfmaw(cat(wl[ks][T], wh[ks][T]), xf[ks][m], (s == 0 && ks == 0) ? zero : acc[T][m]); SBAR(); } };
  STAMP(1);
  static_for<0, 20>([&](auto SS) __attribute__((always_inline)) {
    constexpr int s = decltype(SS)::value;
    wait_vmc(s == 0 ? 2 : s == 1 ? 7 : (s >= 7 && s <= 9) ? 7 : s <= 16 ? 6 : s == 17 ? 5 : s == 18 ? 2 : 0);
    WAIT_LGKM0();
    bar();
    STAMP2(s);
    if (s == 0) STAMP(13);
    SBAR();
    mm(s - 1, 1, NW - 2, 1); rdw(s, 0, 0); rdx(s, 0, 0);
    mm(s - 1, 1, NW - 1, 0); rdw(s, 0, 1); rdx(s, 0, 1);
    mm(s - 1, 1, NW - 1, 1); rdx(s, 0, 2); rdw(s, 1, 0);
    if (s == 0) { issue_x(2); iw(2, 0); iw(2, 1); issue_x(3); SBAR(); }
    mm(s, 0, 0, 0); rdx(s, 1, 0); iw(s + 3, 0); SBAR();
    mm(s, 0, 0, 1); rdw(s, 1, 1);
    mm(s, 0, 1, 0); rdx(s, 1, 1); iw(s + 3, 1); SBAR();
    mm(s, 0, 1, 1); rdx(s, 1, 2);
    mm(s, 0, 2, 0); issue_x(s + 4); SBAR();
    mm(s, 0, 2, 1);
    if (s == 0) {
      LAS unsigned short* wp = (LAS unsigned short*)(L + WPHI + hh * 800);
      const int reg = (lane & 3) + 4 * ((lane >> 3) & 3), f = reg < 10 ? 10 * ((lane >> 2) & 1) + reg : 0;
#pragma unroll
      for (int j = 0; j < 8; ++j) {
        const short a0 = wp[(4 * (2 * (j >> 2) + hf) + (j & 3)) * 20 + f], a1 = wp[(16 + (j & 3)) * 20 + f];
        ap0[j] = reg < 10 ? a0 : (short)0;
        ap1[j] = (reg < 10 && j < 4 && hf == 0) ? a1 : (short)0;
      }
      tag = make_tag(L);
      SBAR();
    }
    if (s == 6) {
      __builtin_amdgcn_raw_buffer_store_b128((u32x4){~0u, ~0u, ~0u, ~0u}, mk_rsrc(P.ws + OFF_KVP, 8 * 13 * KVP_STRIDE),
                                             wid * 64 + lane < 420 ? (hg * 13 + bi) * KVP_STRIDE + (wid * 64 + lane) * 16 : 0x40000000, 0, AUX_SC1);
      SBAR();
    }
    if (NW == 2 && s == 17 && wid == 7) { flag_fetch(P.ws, L + L_FLG, *(LAS int*)(L + L_FOFF + lane * 4)); SBAR(); }
    if (NW == 3 && s == 10 && threadIdx.x == 0) { st_flag(P.ws + OFF_AF + blockIdx.x * 8, tag); st_flag(P.ws + OFF_MF + bi * 128 + hg * 8, tag); }
    SBAR();
    mm(s, 1, 0, 0);
    if (NW == 3) { mm(s, 1, 0, 1); mm(s, 1, 1, 0); }
  });
  mm(19, 1, NW - 2, 1); mm(19, 1, NW - 1, 0); mm(19, 1, NW - 1, 1);
  if (NW == 2 && wid == 7 && !flag_seen(L + L_FLG, lane, tag)) poll(P.ws, p2_flag(p2, lane, 0, hg), tag);
  WAIT_LGKM0();
  bar();
  STAMP(2);
  if (NW == 3) __builtin_amdgcn_s_setprio(1);
  const int va = a_voff(p2, wid ^ 4, lane), v2a = w2_voff(p2, hh, lane), v2 = NW == 2 ? (v2a ^ 64) + 8 * 1280 : v2a;
  auto ring = [&](int n) { glds16(p2.wd, L + w2_slot(n) + (2 * hh + (NW == 2)) * 1024, n < 9 ? v2 : v2 + 64 * 1280, p2.tw[n % 9]); };
  LAS char* img = L + hh * 20480 + (32 * M0 + (lane & 31)) * 128;
  const int sw = lane & 7;
  const rsrc_t rh = mk_rsrc(P.ws + OFF_H, F * SP * 2);
  u32x4 bq1[NW];
  {
    f32x16 u[NW];
    LAS char* dA = hf ? img + ((5 ^ sw) << 4) : L + L_DUMP + lane * 8;
    LAS char* dB = img + (((5 + hf) ^ sw) << 4) + 8 * (1 - hf);
    LAS char* dC = hf ? img + ((6 ^ sw) << 4) + 8 : L + L_DUMP + lane * 8;
    LAS char* dD = img + ((7 ^ sw) << 4) + 8 * hf;
    const int st2 = hf ? 4096 : 0;
    float valid[NW];
#pragma unroll
    for (int m = 0; m < NW; ++m) {
      u32x4 b0, b1;
#pragma unroll
      for (int i = 0; i < 4; ++i) { b0[i] = pk2(acc[0][m][2 * i], acc[0][m][2 * i + 1]); b1[i] = pk2(acc[0][m][8 + 2 * i], acc[0][m][9 + 2 * i]); }
      u[m] = mfmaw(ap1, __builtin_bit_cast(s16x8, b1), mfmaw(ap0, __builtin_bit_cast(s16x8, b0), zero));
      *(LAS u32x2*)(dA + m * st2) = (u32x2){b1[0], b1[1]};
      *(LAS u32x2*)(dB + m * 4096) = (u32x2){b1[2], b1[3]};
      valid[m] = bi * 160 + 32 * (M0 + m) < S ? 1.f : 0.f;
    }
#pragma unroll
    for (int m = 0; m < NW; ++m) {
#pragma unroll
      for (int i = 0; i < 4; ++i) bq1[m][i] = pk2(acc[1][m][8 + 2 * i], acc[1][m][9 + 2 * i]);
      *(LAS u32x2*)(dC + m * st2) = (u32x2){bq1[m][0], bq1[m][1]};
      *(LAS u32x2*)(dD + m * 4096) = (u32x2){hf ? 0x3f80u : bq1[m][2], hf ? 0u : bq1[m][3]};
    }
    SBAR();
    unsigned o[NW][10];
    dual_softmax<NW>(u, valid, o, [&](auto I) __attribute__((always_inline)) {
      constexpr int i = decltype(I)::value;
      if (NW == 3) { if (i % 2 == 0 && i < 16) ring(i / 2); }
      else {
        if (i < 8) { ring(i); A.v[i][0] = __builtin_amdgcn_raw_buffer_load_b128(rh, va, p2.ta[i < 8 ? i : 0], AUX_SC1); }
        if (i >= 6) A.v[i - 6][1] = __builtin_amdgcn_raw_buffer_load_b128(rh, va + 256, p2.ta[i >= 6 ? i - 6 : 0], AUX_SC1);
      }
    });
#pragma unroll
    for (int m = 0; m < NW; ++m) {
      *(LAS u32x4*)(img + m * 4096 + ((hf ^ sw) << 4)) = (u32x4){o[m][0], o[m][1], o[m][2], o[m][3]};
      *(LAS u32x4*)(img + m * 4096 + (((2 + hf) ^ sw) << 4)) = (u32x4){o[m][4], o[m][5], o[m][6], o[m][7]};
      *(LAS u32x2*)(img + m * 4096 + ((4 ^ sw) << 4) + 8 * hf) = (u32x2){o[m][8], o[m][9]};
    }
  }
  STAMP(4);
  WAIT_LGKM0();
  bar();
  STAMP(7);
  {
    constexpr int wm = NW == 3 ? 0 : 1;
    const int t = lane & 15, g = lane >> 4, q = (lane >> 2) & 3, p = lane & 3;
    const f32x4 z4 = {0.f, 0.f, 0.f, 0.f};
    f32x4 kv[3] = {z4, z4, z4};
    const int tk = 16 * (g >> 1) + 4 * (g & 1) + q, sz = 4 * (g & 1) + q;
    LAS char* base = L + hh * 20480 + tk * 128 + 8 * (p & 1);
    LAS char* pa[3];
#pragma unroll
    for (int ft = 0; ft < 3; ++ft) pa[ft] = base + (((2 * ft + (p >> 1)) ^ sz) << 4);
    LAS char* pb = base + (((wm ? 7 : 5 + (p >> 1)) ^ sz) << 4);
    s16x8 fr[2][4];
    auto rd = [&](int st) {
#pragma unroll
      for (int ft = 0; ft < 3; ++ft) fr[st & 1][ft] = cat(lds_tr(pa[ft] + st * 4096), lds_tr(pa[ft] + st * 4096 + 1024));
      fr[st & 1][3] = cat(lds_tr(pb + st * 4096), lds_tr(pb + st * 4096 + 1024));
      SBAR();
    };
    rd(0);
#pragma unroll
    for (int st = 0; st < 5; ++st) {
      if (st < 4) rd(st + 1);
#pragma unroll
      for (int ft = 0; ft < 3; ++ft) kv[ft] = mfma32(fr[st & 1][ft], fr[st & 1][3], kv[ft]);
      SBAR();
    }
    const rsrc_t rk = mk_rsrc(P.ws + OFF_KVP, 8 * 13 * KVP_STRIDE);
    const int vk = (hg * 13 + bi) * KVP_STRIDE + hh * 1680 + (16 * wm + t) * 80 + 8 * g;
#pragma unroll
    for (int ft = 0; ft < 3; ++ft)
      __builtin_amdgcn_raw_buffer_store_b64((u32x2){pk2(kv[ft][0], kv[ft][1]), pk2(kv[ft][2], kv[ft][3])}, rk,
                                            ((wm == 0 || t < 5) && (ft < 2 || g < 2)) ? vk + 32 * ft : 0x40000000, 0, AUX_SC1);
  }
  STAMP(11);
  WAIT_LGKM0();
  bar();
  u32x4 kvp[13];
  unsigned pq[NW][10];
  {
    f32x16 u[NW];
#pragma unroll
    for (int m = 0; m < NW; ++m) {
      u32x4 b0;
#pragma unroll
      for (int i = 0; i < 4; ++i) b0[i] = pk2(acc[1][m][2 * i], acc[1][m][2 * i + 1]);
      u[m] = mfmaw(ap1, __builtin_bit_cast(s16x8, bq1[m]), mfmaw(ap0, __builtin_bit_cast(s16x8, b0), zero));
    }
    SBAR();
    dual_softmax<NW>(u, nullptr, pq, [&](auto I) __attribute__((always_inline)) {
      constexpr int i = decltype(I)::value;
      if (NW == 3) { if (i % 2 == 0 && i < 16) ring(8 + i / 2); } else if (i < 8) ring(8 + i);
    });
  }
  if (NW == 3 && wid == 3) flag_fetch(P.ws, L + L_FLG + 512, *(LAS int*)(L + L_FOFF + 256 + lane * 4));
  __builtin_amdgcn_s_setprio(0);
  att_fetch(P, hg, wid * 64 + lane, kvp);
  if (NW == 2 && wid == 6) poll(P.ws, OFF_PF + (lane & 7) * 8, tag);
  STAMP(5);
  att_finish<NW>(P, L, bi, hg, wid, lane, kvp, pq);
}
DI void gate_block(const Params& P, LAS char* L, int sp, int wid, int lane, const P2& p2, u64& tag, AFrag& A) {
  {
      wait_vm<0>();
    bar();
    tag = make_tag(L);
    if (threadIdx.x == 0) st_flag(P.ws + OFF_AF + blockIdx.x * 8, tag);
  }
  const rsrc_t rx = mk_rsrc(P.x, S * D * 2), rg = mk_rsrc(P.gw, D * 2), rG = mk_rsrc(P.ws + OFF_G, S * 4);
  const int t = lane & 15, g = lane >> 4;
  u32x4 gw[5];
#pragma unroll
  for (int i = 0; i < 5; ++i) gw[i] = __builtin_amdgcn_raw_buffer_load_b128(rg, t * 16, i * 256, 0);
  const int row = sp * 256 + wid * 32 + g;
#pragma unroll 2
  for (int rr = 0; rr < 8; ++rr) {
    u32x4 xv[5];
#pragma unroll
    for (int i = 0; i < 5; ++i) xv[i] = __builtin_amdgcn_raw_buffer_load_b128(rx, (row + 4 * rr) * 1280 + t * 16, i * 256, 0);
    float a = 0.f;
#pragma unroll
    for (int i = 0; i < 5; ++i)
#pragma unroll
      for (int e = 0; e < 4; ++e) { a = __builtin_fmaf(blo(xv[i][e]), blo(gw[i][e]), a); a = __builtin_fmaf(bhi(xv[i][e]), bhi(gw[i][e]), a); }
    a += __shfl_xor(a, 1); a += __shfl_xor(a, 2); a += __shfl_xor(a, 4); a += __shfl_xor(a, 8);
    const float s = __builtin_amdgcn_rcpf(1.f + __builtin_amdgcn_exp2f(a * -1.44269504089f));
    if (t == 0) __builtin_amdgcn_raw_buffer_store_b32(__float_as_uint(s), rG, (row + 4 * rr) * 4, 0, AUX_SC1);
  }
  drain_vm();
  bar();
  if (threadIdx.x == 0) st_flag(P.ws + OFF_GF + sp * 8, tag);
  STAMP(14);
  {
    const rsrc_t ro = mk_rsrc(P.ws + OFF_O, D * SP * 2);
#pragma unroll 4
    for (int i = 0; i < 40; ++i) __builtin_amdgcn_raw_buffer_store_b128((u32x4){~0u, ~0u, ~0u, ~0u}, ro, sp * 327680 + i * 8192 + (wid * 64 + lane) * 16, 0, AUX_SC1);
    drain_vm();
    bar();
    if (threadIdx.x == 0) st_flag(P.ws + OFF_PF + sp * 8, tag);
    STAMP(15);
  }
  if (wid < 4) {
    const int v2 = w2_voff(p2, wid, lane);
#pragma unroll
    for (int n = 0; n < W2_PRE; ++n) { w2_piece(p2, L, n, wid, 0, n < 9 ? v2 : v2 + 64 * 1280, p2.tw[n % 9]); w2_piece(p2, L, n, wid, 1, n < 9 ? v2 : v2 + 64 * 1280, p2.tw[n % 9]); }
  }
  if (sp == 0 && wid == 0) {
#pragma unroll
    for (int i = 0; i < 4; ++i) poll(P.ws, OFF_AF + (i * 64 + lane) * 8, tag);
    if (lane == 0) st_flag(P.ws + OFF_E, tag);
  }
  if (wid == 7 && p2.on) { poll(P.ws, p2_flag(p2, lane, 0), tag); poll(P.ws, p2_flag(p2, lane, 1), tag); }
  drain_vm();
  bar();
  const rsrc_t rh = mk_rsrc(P.ws + OFF_H, F * SP * 2);
  const int va = a_voff(p2, wid, lane);
#pragma unroll
  for (int n = 0; n < AS; ++n)
#pragma unroll
    for (int m = 0; m < 3; ++m) A.v[n][m] = __builtin_amdgcn_raw_buffer_load_b128(rh, va + m * 256, p2.ta[n], AUX_SC1);
}

#define S_U2 10
#define S_O 19
DI void reload16(u32x4& d, int voff, srd_t r, int soff, int imm) { asm volatile("buffer_load_dwordx4 %0, %1, %2, %3 offen offset:%4 sc1" : "+v"(d) : "v"(voff), "s"(r), "s"(soff), "i"(imm) : "memory"); }
DI void drain_into(u32x4& d) { asm volatile("s_waitcnt vmcnt(0)" : "+v"(d)::"memory"); }
DI constexpr int a_issue_stage(int n) { return n >= 27 ? (n - AS > S_O ? n - AS : S_O) : n >= 18 ? (n - AS > S_U2 ? n - AS : S_U2) : n - AS; }

DI constexpr int ring_pos(int s) { return s == 0 ? 16 : s == 2 ? 17 : s >= 4 ? s + 14 : 99; }
DI void out_loader(const Params& P, LAS char* L, const P2& p2, int role, int wid, int lane, u64 tag) {
  int vw[3], tw[10];
#pragma unroll
  for (int u = 0; u < 3; ++u) vw[u] = w2_voff(p2, wid & 3, lane) + u * 64 * 1280;
#pragma unroll
  for (int j = 0; j < 9; ++j) tw[j] = p2.tw[j];
  if (wid == 7 && role != 2) {
    if (role == 0) wait_vm<14>();
    if (!flag_seen(L + L_FLG + 512, lane, tag)) poll(P.ws, p2_flag(p2, lane, 1), tag);
  }
  static_for<0, 10>([&](auto KK) __attribute__((always_inline)) {
    constexpr int k = decltype(KK)::value;
    constexpr int issued = k == 0 ? 0 : (4 * k + 14 < W2_STAGES ? 4 * k + 14 : W2_STAGES) - 16, need = 4 * k + 5 - 16 > 0 ? (4 * k + 5 < W2_STAGES ? 4 * k + 5 : W2_STAGES) - 16 : 0;
    if (k == 1) { if (role == 0) wait_vmc(2 * issued + 2); }
    else if (need > 0) wait_vmc(2 * (issued - need));
    if (k == 4 && wid == 7 && role != 1) {
      wait_vm<16>();
      if (!flag_seen(L + L_FLG, lane, tag)) poll(P.ws, OFF_PF + (lane & 7) * 8, tag);
    }
    bar();
    if (k == 2 && wid == 7 && role != 1) flag_fetch(P.ws, L + L_FLG, OFF_PF + (lane & 7) * 8);
    if (k == 3) p2_table<10>(p2, tw + 0, 27, 64 * 1280);
#pragma unroll
    for (int s = 4 * k; s < 4 * k + 4; ++s) {
      const int rp = ring_pos(s);
      if (rp < W2_STAGES && !(DBG & 2)) {
        const int so = (k == 3 && rp < 27) ? p2.tw[rp % 9] : tw[rp < 27 ? rp % 9 : rp - 27];
        w2_piece(p2, L, rp, wid & 3, 0, vw[rp < 27 ? rp / 9 : 0], so); w2_piece(p2, L, rp, wid & 3, 1, vw[rp < 27 ? rp / 9 : 0], so);
      }
    }
  });
  bar();
}

template <int NM>
DI void out_wave(const Params& P, LAS char* L, const P2& p2, int role, int wid, int lane, u64 tag, AFrag& A) {
  const int g = lane >> 4, q = (lane >> 2) & 3, kh = wid & 1;
  const int tok0 = p2_tok(p2, wid);
  int va[3];
#pragma unroll
  for (int u = 0; u < 3; ++u) {
    va[u] = a_voff(p2, wid, lane) + u * 8 * SP * 16;
    asm volatile("" : "+v"(va[u]));
  }
  int to[10];
  const rsrc_t rh = mk_rsrc(P.ws + OFF_H, F * SP * 2), ro = mk_rsrc(P.ws + OFF_O, D * SP * 2);
  LAS char* wb[3][4];
  {
    const int sw = ((q >> 1) & 1) | ((g & 1) << 1);
#pragma unroll
    for (int j = 0; j < 3; ++j)
#pragma unroll
      for (int n = 0; n < 4; ++n) { wb[j][n] = L + j * 57344 + kh * 4096 + (8 * g + q) * 128 + ((n ^ sw) << 5) + 8 * (lane & 3); asm volatile("" : "+v"(wb[j][n])); }
  }
  const f32x4 zero = {0.f, 0.f, 0.f, 0.f};
  f32x4 acc[4][NM];
  float gate[NM];
  s16x4 wl[2][4], wh[2][4];
  auto rdw = [&](int s, int n, int h) {
    if (s < W2_STAGES) {
      LAS char* b = wb[w2_slot(s) / 57344][n] + w2_slot(s) % 57344;
      if (h) wh[s & 1][n] = lds_tr(b + 512); else wl[s & 1][n] = lds_tr(b);
    }
    SBAR();
  };
  STAMP(8);
  static_for<0, W2_STAGES>([&](auto SS) __attribute__((always_inline)) {
    constexpr int s = decltype(SS)::value;
    if ((s & 3) == 0) {
      if (s == 4 && role == 0) wait_vmc(4 * NM + 26);
      bar();
    }
    if (s >= 9 && s < 19) { to[s - 9] = p2_stage(p2, 27 + s - 9) * 8 * SP * 16; asm volatile("" : "+s"(to[s >= 9 && s < 19 ? s - 9 : 0])); }
    STAMP2(64 + s);
    if (s == 0) {
#pragma unroll
      for (int n = 0; n < 4; ++n) { rdw(0, n, 0); rdw(0, n, 1); }
    }
    if (s == 22) {
#pragma unroll
      for (int m = 0; m < NM; ++m) gate[m] = __uint_as_float(__builtin_amdgcn_raw_buffer_load_b32(mk_rsrc(P.ws + OFF_G, S * 4), (tok0 + 16 * m + (lane_id() & 15)) * 4, 0, AUX_SC1));
    }
    if (s == 27) {
#pragma unroll
      for (int m = 0; m < NM; ++m) {
        gate[m] = __builtin_fmaxf(gate[m], 1e-30f);
        const float sc = (1.f - gate[m]) * __builtin_amdgcn_rcpf(gate[m]);
#pragma unroll
        for (int n = 0; n < 4; ++n) acc[n][m] *= sc;
      }
    }
    SBAR();
    auto mm = [&](int n, int m) {
      if (m < NM) {
        const s16x8 af = __builtin_bit_cast(s16x8, A.v[s % (AS + 1)][m]), w = cat(wl[s & 1][n], wh[s & 1][n]);
        acc[n][m < NM ? m : 0] = mfma32(w, af, s == 0 ? zero : acc[n][m < NM ? m : 0]);
        SBAR();
      }
    };
    auto lda = [&](int m) {
      if (m < NM) {
#pragma unroll
        for (int pos = AS; pos < W2_STAGES; ++pos)
          if (a_issue_stage(pos) == s && !(DBG & 1))
            A.v[pos % (AS + 1)][m] = __builtin_amdgcn_raw_buffer_load_b128(pos < 27 ? rh : ro, va[pos < 27 ? pos / 9 : 0] + m * 256, pos < 27 ? p2.ta[pos % 9] : to[pos < 27 ? 0 : pos - 27], AUX_SC1);
        SBAR();
      }
    };
    mm(0, 0); rdw(s + 1, 0, 0); mm(0, 1); rdw(s + 1, 0, 1); mm(0, 2); lda(0);
    mm(1, 0); rdw(s + 1, 1, 0); mm(1, 1); rdw(s + 1, 1, 1); mm(1, 2); lda(1);
    mm(2, 0); rdw(s + 1, 2, 0); mm(2, 1); rdw(s + 1, 2, 1); mm(2, 2);
    mm(3, 0); rdw(s + 1, 3, 0); mm(3, 1); rdw(s + 1, 3, 1); mm(3, 2); lda(2);
    if constexpr (s + 1 >= 18 && s + 1 < W2_STAGES && !(DBG & 4)) {
      auto marked = [&]() {
        unsigned mx = 0u;
#pragma unroll
        for (int m = 0; m < NM; ++m) mx = umax3(mx, A.v[(s + 1) % (AS + 1)][m][0], A.v[(s + 1) % (AS + 1)][m][2]);
        return __any(mx == 0xffffffffu);
      };
      if (marked()) {
        unsigned it = 0;
        do {
          static_for<0, 3>([&](auto JJ) __attribute__((always_inline)) {
            constexpr int pos = s + 1 + decltype(JJ)::value;
            if constexpr (pos < W2_STAGES && a_issue_stage(pos < W2_STAGES ? pos : 0) <= s) {
              const srd_t r = pos < 27 ? mk_srd(P.ws + OFF_H, F * SP * 2) : mk_srd(P.ws + OFF_O, D * SP * 2);
              const int so = p2_stage(p2, pos) * 8 * SP * 16;
#pragma unroll
              for (int m = 0; m < NM; ++m) reload16(A.v[pos % (AS + 1)][m], va[0], r, so, m * 256);
            }
            SBAR();
          });
#pragma unroll
          for (int m = 0; m < NM; ++m) drain_into(A.v[(s + 1) % (AS + 1)][m]);
          SBAR();
        } while (marked() && ++it < (1u << 14));
      }
    }
  });
  STAMP(9);
  const int l2 = lane_id();
  f32x4 kp[2][NM];
  if (kh) {
#pragma unroll
    for (int e = 0; e < 2; ++e)
#pragma unroll
      for (int m = 0; m < NM; ++m) { *(LAS f32x4*)(L + wid * 8192 + (e * NM + m) * 1024 + l2 * 16) = acc[e][m]; kp[e][m] = acc[2 + e][m]; }
  } else {
#pragma unroll
    for (int e = 0; e < 2; ++e)
#pragma unroll
      for (int m = 0; m < NM; ++m) { *(LAS f32x4*)(L + wid * 8192 + (e * NM + m) * 1024 + l2 * 16) = acc[2 + e][m]; kp[e][m] = acc[e][m]; }
  }
  WAIT_LGKM0();
  bar();
  const rsrc_t rout = mk_rsrc(P.out, S * D * 2);
  const int vout = (tok0 + (l2 & 15)) * 1280 + p2.c * 128 + 64 * kh + ((l2 >> 4) & 1) * 32 + (l2 >> 5) * 16;
#pragma unroll
  for (int m = 0; m < NM; ++m) {
    unsigned w[2][2];
#pragma unroll
    for (int e = 0; e < 2; ++e) {
      const f32x4 r = (kp[e][m] + *(LAS f32x4*)(L + (wid ^ 1) * 8192 + (e * NM + m) * 1024 + l2 * 16)) * gate[m];
      w[e][0] = pk2(r[0], r[1]); w[e][1] = pk2(r[2], r[3]);
    }
    const auto r0 = __builtin_amdgcn_permlane16_swap(w[0][0], w[1][0], false, false);
    const auto r1 = __builtin_amdgcn_permlane16_swap(w[0][1], w[1][1], false, false);
    __builtin_amdgcn_raw_buffer_store_b128((u32x4){r0[0], r1[0], r0[1], r1[1]}, rout, vout + m * 16 * 1280, 0, AUX_SC1);
  }
#if !(DBG & 256)
  if constexpr (NM == 3) asm volatile(".Lcode_tail3:"); else asm volatile(".Lcode_tail2:");
#endif
  STAMP(10);
  DUMP2();
}

__global__ __launch_bounds__(512, 2) void fused_kernel(Params P) {
  extern __shared__ __attribute__((aligned(16))) char smem[];
  LAS char* L = (LAS char*)smem;
  const int lane = threadIdx.x & 63, wid = __builtin_amdgcn_readfirstlane(threadIdx.x >> 6);
  const int b = blockIdx.x, xcd = b & 7, slot = b >> 3;
#if !(DBG & 256)
  if (wid == 0) {
    u64 pc; int len, len2;
    asm volatile("s_getpc_b64 %0\n.Lcode_here:\n\ts_mov_b32 %1, .Lcode_tail3-.Lcode_here\n\ts_mov_b32 %2, .Lcode_tail2-.Lcode_here\n\ts_max_i32 %1, %1, %2\n\ts_mov_b32 %2, .Lcode_tail1-.Lcode_here\n\ts_max_i32 %1, %1, %2\n\ts_max_i32 %1, %1, 0" : "=s"(pc), "=s"(len), "=s"(len2) :: "scc");
    const srd_t rc = {(int)(unsigned)pc, (int)((unsigned)(pc >> 32) & 0xffffu), len, 0x00020000};
    asm volatile("s_mov_b32 m0, %0\n\ts_nop 0\n\tbuffer_load_dword %1, %2, 0 offen lds" ::"s"((unsigned)(uintptr_t)(L + L_DUMP)), "v"((slot * 64 + lane) * 128), "s"(rc) : "memory");
  }
#endif
  STAMP(0);
  int role, bi, bj, idx;
  if (xcd < 6) {
    const int pp = xcd & 1, ss = xcd >> 1;
    if (slot < 24) { role = 0; bi = 8 * pp + (slot & 7); bj = 3 * ss + (slot >> 3); idx = 50 * ss + 24 * pp + slot; }
    else if (pp == 0) { role = 1; if (slot < 31) { bj = ss; bi = slot - 24; } else { bj = 7; bi = 4 + ss; } idx = 14 * ss + slot - 24; }
    else if (slot < 30) { role = 1; bj = ss; bi = 7 + slot - 24; idx = 14 * ss + 8 + slot - 24; }
    else { role = 2; bi = 2 * ss + slot - 30; bj = 0; idx = 50 * ss + 24 + slot - 6; }
  } else if (xcd == 6) { role = 1; bj = 3 + slot / 7; bi = slot % 7; idx = 42 + slot; }
  else if (slot < 30) { role = 1; bj = 3 + slot / 6; bi = 7 + slot % 6; idx = 74 + slot; }
  else { role = 2; bi = 6 + slot - 30; bj = 0; idx = 150 + slot - 30; }
  role = __builtin_amdgcn_readfirstlane(role); bi = __builtin_amdgcn_readfirstlane(bi); bj = __builtin_amdgcn_readfirstlane(bj); idx = __builtin_amdgcn_readfirstlane(idx);
  P2 p2;
  {
    const int grp = idx / 10;
    p2.c = idx - grp * 10;
    p2.on = role == 1 ? idx < 100 : idx < 150;
    p2.nm = role == 1 ? 2 : 3;
    p2.row0 = role == 1 ? 1440 + 64 * grp : 96 * grp;
    p2.ra = (p2.c + 3 * grp) % 9; p2.ro = (p2.c + 3 * grp) % 10;
    int a = p2.ra;
#pragma unroll
    for (int j = 0; j < 9; ++j) {
      p2.ta[j] = a * 3 * 8 * SP * 16; p2.tw[j] = a * 3 * 64 * 1280;
      asm volatile("" : "+s"(p2.ta[j]), "+s"(p2.tw[j]));
      a = a == 8 ? 0 : a + 1;
    }
    p2.wd = mk_srd(P.wd, F * D * 2); p2.wo = mk_srd(P.wo, D * D * 2);
  }
  u64 tag = 0;
  fetch_epoch(P.ws, L, wid, lane);
  if (role == 1) {
    AFrag A;
    if (wid < 4) {
      att_block<3>(P, L, bi, bj, wid, lane, p2, tag, A);
      if (p2.on) out_loader(P, L, p2, 1, wid + 4, lane_id(), tag); else { DUMP2(); }
      asm volatile(".Lcode_tail1:");
    } else {
      att_block<2>(P, L, bi, bj, wid, lane, p2, tag, A);
      if (p2.on) out_wave<2>(P, L, p2, 1, wid - 4, lane_id(), tag, A);
    }
  } else {
    AFrag A;
    if (role == 0) ffn_block(P, L, bi, bj, wid, lane, p2, tag, A);
    else gate_block(P, L, bi, wid, lane, p2, tag, A);
    if (p2.on) {
      if (wid >= 4) out_loader(P, L, p2, role, wid, lane_id(), tag);
      else out_wave<3>(P, L, p2, role, wid, lane_id(), tag, A);
    }
  }
}

extern "C" void kernel_launch(void* const* d_in, const int* in_sizes, int n_in, void* d_out, int out_size, void* d_ws, size_t ws_size, hipStream_t stream) {
  Params P;
  P.x = (const char*)d_in[0]; P.wqkv = (const char*)d_in[1]; P.wphi = (const char*)d_in[2]; P.wo = (const char*)d_in[3];
  P.wg = (const char*)d_in[5]; P.wu = (const char*)d_in[6]; P.wd = (const char*)d_in[7]; P.gw = (const char*)d_in[8];
  P.out = (char*)d_out; P.ws = (char*)d_ws;
  (void)hipFuncSetAttribute((const void*)fused_kernel, hipFuncAttributeMaxDynamicSharedMemorySize, LDS_BYTES);
  fused_kernel<<<256, 512, LDS_BYTES, stream>>>(P);
}
```
